```python
import torch

def _softmax(x, axis=-1):
    return torch.softmax(x, dim=axis)
def _gelu(x):
    return torch.nn.functional.gelu(x, approximate="tanh")
def _silu(x):
    return torch.nn.functional.silu(x)
def _sigmoid(x):
    return torch.sigmoid(x)
def _layernorm(x, axis=-1, eps=1e-5):
    m = x.mean(dim=axis, keepdim=True)
    v = x.var(dim=axis, keepdim=True, unbiased=False)
    return (x - m) / torch.sqrt(v + eps)
def _scatter_add(tgt, idx, src, axis=0):
    return tgt.clone().index_add_(axis, idx, src)
def _segment_sum(src, seg, n):
    out = torch.zeros((n,) + src.shape[1:], dtype=src.dtype)
    return out.index_add_(0, seg, src)
def _pad(x, pw):
    flat = tuple(v for pair in reversed(pw) for v in pair)
    return torch.nn.functional.pad(x, flat)

kernel_name = 'gated_mix__mha_block_cached__swiglu_ffn__decode'

def setup_inputs():

    _w = (lambda a: (lambda t: t.to(torch.bfloat16) if t.is_floating_point() else t)(torch.as_tensor(a)))
    def _A_setup():
        B, Sq, D = 8, 1, 448
        H, Sk, Dh = 16, 4096, 256
        return {
            "x": _w((lambda *s: torch.randn(*s, dtype=torch.bfloat16) * 0.05)(B, Sq, D)),
            "k_cache": _w((lambda *s: torch.randn(*s, dtype=torch.bfloat16) * 6.4)(B, H, Sk, Dh)),
            "v_cache": _w((lambda *s: torch.randn(*s, dtype=torch.bfloat16) * 0.2)(B, H, Sk, Dh)),
            "wq": _w((lambda *s: torch.randn(*s, dtype=torch.bfloat16) * 6.4)(D, H * Dh) * (D ** -0.5)),
            "wo": _w((lambda *s: torch.randn(*s, dtype=torch.bfloat16) * 0.2)(H * Dh, D) * ((H * Dh) ** -0.5)),
        }
    _A_in = _A_setup()

    def _B_setup():
        B, S, D, F = 8, 1, 448, 1792
        return {
            "x": _w((lambda *s: torch.randn(*s, dtype=torch.bfloat16) * 0.05)(B, S, D)),
            "w_gate": _w((lambda *s: torch.randn(*s, dtype=torch.bfloat16) * 0.05)(D, F)),
            "w_up": _w((lambda *s: torch.randn(*s, dtype=torch.bfloat16) * 0.05)(D, F)),
            "w_down": _w((lambda *s: torch.randn(*s, dtype=torch.bfloat16) * 0.05)(F, D)),
        }
    _B_in = _B_setup()

    return {
        "A_x": _A_in["x"],
        "A_k_cache": _A_in["k_cache"],
        "A_v_cache": _A_in["v_cache"],
        "A_wq": _A_in["wq"],
        "A_wo": _A_in["wo"],
        "B_x": _B_in["x"],
        "B_w_gate": _B_in["w_gate"],
        "B_w_up": _B_in["w_up"],
        "B_w_down": _B_in["w_down"],
        "gate_w": _w((lambda *s: torch.randn(*s, dtype=torch.bfloat16) * 0.05)(448) * 16.0),
    }

def reference(A_x, A_k_cache, A_v_cache, A_wq, A_wo, B_x, B_w_gate, B_w_up, B_w_down, gate_w):
    def _A_ref(x, k_cache, v_cache, wq, wo):
        B, Sq, D = x.shape
        H, Dh = 16, 256
        q = (lambda x, *d: x.permute(*d))((x @ wq).reshape(B, Sq, H, Dh), 0, 2, 1, 3)
        scores = torch.einsum("bhqd,bhkd->bhqk", q, k_cache) * (Dh ** -0.5)
        p = _softmax(scores, axis=-1)
        o = torch.einsum("bhqk,bhkd->bhqd", p, v_cache)
        return (lambda x, *d: x.permute(*d))(o, 0, 2, 1, 3).reshape(B, Sq, H * Dh) @ wo

    def _B_ref(x, w_gate, w_up, w_down):
        g = _silu(x @ w_gate)
        u = x @ w_up
        return (g * u) @ w_down

    x = A_x
    g = _sigmoid((x * gate_w).sum(-1, keepdims=True))
    a = _A_ref(x, A_k_cache, A_v_cache, A_wq, A_wo)
    b = _B_ref(x, B_w_gate, B_w_up, B_w_down)
    return g * a + (1 - g) * b
```

```cpp
#include <hip/hip_runtime.h>
#include <hip/hip_bf16.h>
#include <stdint.h>

namespace {

constexpr int NB = 8, DM = 448, NH = 16, SK = 4096, DH = 256, FF = 1792;
constexpr int NWG = 256;
constexpr int NSTREAM = 4, NHELP = 8, NTHR = (NSTREAM + NHELP) * 64;
constexpr int RING = 32;
constexpr int ROWS = 256;
constexpr int NIT = ROWS / RING;
constexpr int GRP = 32;
constexpr int UPW = FF / GRP;
constexpr int CPW = DM / GRP;
constexpr float SC2 = 0.0625f * 1.4426950408889634f;
constexpr unsigned SPIN_CAP = 1u << 20;

constexpr size_t WS_EPOCH = 0;
constexpr size_t WS_STAT = 4096;
constexpr size_t WS_Y = WS_STAT + (size_t)NWG * 128;
constexpr size_t WS_END = WS_Y + (size_t)NWG * DM * 8;

typedef unsigned u32;
typedef unsigned long long u64;
typedef u32 v4u __attribute__((ext_vector_type(4)));
typedef u32 v2u __attribute__((ext_vector_type(2)));
typedef float f32x4 __attribute__((ext_vector_type(4)));
typedef float f32x2 __attribute__((ext_vector_type(2)));
typedef __bf16 bf16x2 __attribute__((ext_vector_type(2)));
#define GAS __attribute__((address_space(1)))
#define LAS __attribute__((address_space(3)))
#define RLX_AGENT __ATOMIC_RELAXED, __HIP_MEMORY_SCOPE_AGENT
#define RLX_WG __ATOMIC_RELAXED, __HIP_MEMORY_SCOPE_WORKGROUP
typedef __amdgpu_buffer_rsrc_t rsrc_t;

struct __attribute__((aligned(16))) Lds {
  float qpart[NHELP][DH];
  float pbuf[NSTREAM][NIT * 64];
  float ow[NSTREAM][DH];
  float yw[NHELP][DM];
  float fpart[NHELP][128];
  float xs[DM];
  float stat[NSTREAM][2];
  float fac[NSTREAM];
  u32 cnt_stat;
  u32 cnt_help;
  u32 pad[2];
};

__device__ __forceinline__ rsrc_t mk_rsrc(const void* p, u32 bytes) {
  return __builtin_amdgcn_make_buffer_rsrc(const_cast<void*>(p), 0, (int)bytes, 0x00020000);
}
__device__ __forceinline__ v4u ld16(rsrc_t r, int voff, int soff) { return __builtin_amdgcn_raw_buffer_load_b128(r, voff, soff, 0); }
__device__ __forceinline__ v4u ld16nt(rsrc_t r, int voff, int soff) { return __builtin_amdgcn_raw_buffer_load_b128(r, voff, soff, 2); }
__device__ __forceinline__ float dot2(u32 a, u32 b, float acc) {
  return __builtin_amdgcn_fdot2_f32_bf16(__builtin_bit_cast(bf16x2, a), __builtin_bit_cast(bf16x2, b), acc, false);
}
__device__ __forceinline__ float bf_lo(u32 v) { return __builtin_bit_cast(float, v << 16); }
__device__ __forceinline__ float bf_hi(u32 v) { return __builtin_bit_cast(float, v & 0xffff0000u); }
__device__ __forceinline__ u32 cvtpk(float lo, float hi) {
  const bf16x2 r = {(__bf16)lo, (__bf16)hi};
  return __builtin_bit_cast(u32, r);
}
__device__ __forceinline__ u32 pair_lo(u32 r0, u32 r1) { return __builtin_amdgcn_perm(r1, r0, 0x05040100u); }
__device__ __forceinline__ u32 pair_hi(u32 r0, u32 r1) { return __builtin_amdgcn_perm(r1, r0, 0x07060302u); }
template <int CTRL> __device__ __forceinline__ float dpp(float x) {
  return __builtin_bit_cast(float, __builtin_amdgcn_mov_dpp(__builtin_bit_cast(int, x), CTRL, 0xf, 0xf, true));
}
constexpr int XOR1 = 0xB1, XOR2 = 0x4E, XOR7 = 0x141, XOR8 = 0x128;
__device__ __forceinline__ float swap16_add(float a, float b) {
  auto s = __builtin_amdgcn_permlane16_swap(__float_as_uint(a), __float_as_uint(b), false, false);
  return __uint_as_float(s[0]) + __uint_as_float(s[1]);
}
__device__ __forceinline__ float swap32_add(float a, float b) {
  auto s = __builtin_amdgcn_permlane32_swap(__float_as_uint(a), __float_as_uint(b), false, false);
  return __uint_as_float(s[0]) + __uint_as_float(s[1]);
}
__device__ __forceinline__ float swap16_max(float a) {
  auto s = __builtin_amdgcn_permlane16_swap(__float_as_uint(a), __float_as_uint(a), false, false);
  return fmaxf(__uint_as_float(s[0]), __uint_as_float(s[1]));
}
__device__ __forceinline__ float swap32_max(float a) {
  auto s = __builtin_amdgcn_permlane32_swap(__float_as_uint(a), __float_as_uint(a), false, false);
  return fmaxf(__uint_as_float(s[0]), __uint_as_float(s[1]));
}
__device__ __forceinline__ float sum32(float v) {
  v += dpp<XOR1>(v); v += dpp<XOR2>(v); v += dpp<XOR7>(v); v += dpp<XOR8>(v);
  return swap16_add(v, v);
}
__device__ __forceinline__ float sum64(float v) { v = sum32(v); return swap32_add(v, v); }
__device__ __forceinline__ float max64(float v) {
  v = fmaxf(v, dpp<XOR1>(v)); v = fmaxf(v, dpp<XOR2>(v)); v = fmaxf(v, dpp<XOR7>(v)); v = fmaxf(v, dpp<XOR8>(v));
  return swap32_max(swap16_max(v));
}
__device__ __forceinline__ float rdlane(float v, int l) { return __builtin_bit_cast(float, __builtin_amdgcn_readlane(__builtin_bit_cast(int, v), l)); }
__device__ __forceinline__ void wg_barrier() { asm volatile("s_waitcnt lgkmcnt(0)\n\ts_barrier" ::: "memory"); }
__device__ __forceinline__ u32 lds_ld(LAS u32* p) { return __hip_atomic_load(p, RLX_WG); }
__device__ __forceinline__ float sigmoidf(float z) { return 1.0f / (1.0f + __expf(-z)); }

__device__ __forceinline__ void help_barrier(LAS u32* cnt, u32 target, int lane) {
  asm volatile("s_waitcnt lgkmcnt(0)" ::: "memory");
  if (lane == 0) __hip_atomic_fetch_add(cnt, 1u, RLX_WG);
  for (u32 spins = 0; spins < SPIN_CAP; ++spins) {
    if (__builtin_amdgcn_readfirstlane(lds_ld(cnt)) >= target) break;
    __builtin_amdgcn_s_sleep(1);
  }
  asm volatile("" ::: "memory");
}

#define PIN() __builtin_amdgcn_sched_barrier(0)

__global__ void __launch_bounds__(NTHR, 3)
fused_decode(const u32* __restrict__ x, const u32* __restrict__ kc, const u32* __restrict__ vc, const u32* __restrict__ wq,
             const u32* __restrict__ wo, const u32* __restrict__ wg, const u32* __restrict__ wu, const u32* __restrict__ wd,
             const u32* __restrict__ gw, unsigned short* __restrict__ out, unsigned char* __restrict__ ws) {
  __shared__ Lds lds_;
  LAS Lds* L = (LAS Lds*)&lds_;
  const int tid = threadIdx.x, lane = tid & 63, wave = __builtin_amdgcn_readfirstlane(tid >> 6);
  const int blk = blockIdx.x, b = blk >> 5, h = (blk >> 1) & 15, j = blk & 31;
  const int hf = lane >> 5, c = lane & 31;

  if (wave < NSTREAM) {
    const int w = wave;
    const rsrc_t rk = mk_rsrc((const char*)kc + (size_t)blk * (ROWS * 4096), ROWS * 4096);
    const rsrc_t rv = mk_rsrc((const char*)vc + (size_t)blk * (ROWS * 4096), ROWS * 4096);
    const int voff = lane * 16 + w * 1024;
    v4u a[RING];
#pragma unroll
    for (int i = 0; i < RING; ++i) a[i] = ld16nt(rk, voff, i * 4096);
    PIN();
    wg_barrier();
    u32 q[4];
    {
      f32x4 s0 = {0.f, 0.f, 0.f, 0.f}, s1 = {0.f, 0.f, 0.f, 0.f};
#pragma unroll
      for (int k = 0; k < NHELP; ++k) {
        s0 += *(LAS f32x4*)&L->qpart[k][8 * c];
        s1 += *(LAS f32x4*)&L->qpart[k][8 * c + 4];
      }
      q[0] = cvtpk(s0.x, s0.y); q[1] = cvtpk(s0.z, s0.w); q[2] = cvtpk(s1.x, s1.y); q[3] = cvtpk(s1.z, s1.w);
    }
    float mrun = -INFINITY;
    for (int t = 0; t < NIT; ++t) {
      const bool last = t == NIT - 1;
      const rsrc_t rn = last ? rv : rk;
      const int nb = last ? 0 : (t + 1) * (RING * 4096);
      float part[2];
#pragma unroll
      for (int g2 = 0; g2 < 2; ++g2) {
        float xx[16];
#pragma unroll
        for (int i = 0; i < 16; ++i) {
          const v4u kk = a[g2 * 16 + i];
          float d = dot2(kk.x, q[0], 0.f);
          d = dot2(kk.y, q[1], d); d = dot2(kk.z, q[2], d); d = dot2(kk.w, q[3], d);
          xx[i] = d;
          asm volatile("" : "+v"(xx[i]));
          a[g2 * 16 + i] = ld16nt(rn, voff, nb + (g2 * 16 + i) * 4096);
          PIN();
        }
#pragma unroll
        for (int k = 0; k < 8; ++k) xx[k] = swap16_add(xx[k], xx[k + 8]);
#define TR_STEP(HALF, CTRL, BIT) _Pragma("unroll") for (int k = 0; k < (HALF); ++k) { \
          const float t1 = xx[k] + dpp<CTRL>(xx[k]), t2 = xx[k + (HALF)] + dpp<CTRL>(xx[k + (HALF)]); xx[k] = (c & (BIT)) ? t2 : t1; }
        TR_STEP(4, XOR8, 8) TR_STEP(2, XOR7, 4) TR_STEP(1, XOR2, 2)
#undef TR_STEP
        part[g2] = xx[0];
        PIN();
      }
      const float t1 = part[0] + dpp<XOR1>(part[0]), t2 = part[1] + dpp<XOR1>(part[1]);
      const float sc = (c & 1) ? t2 : t1;
      L->pbuf[w][t * 64 + hf * 32 + (c >> 1) + 16 * (c & 1)] = sc;
      mrun = fmaxf(mrun, sc);
      PIN();
    }
    {
      const float m2 = max64(mrun) * SC2;
      float ls = 0.f;
#pragma unroll
      for (int t = 0; t < NIT; ++t) {
        const float p = __builtin_amdgcn_exp2f(fmaf(L->pbuf[w][t * 64 + lane], SC2, -m2));
        L->pbuf[w][t * 64 + lane] = p;
        ls += p;
      }
      ls = sum64(ls);
      if (lane == 0) { L->stat[w][0] = m2; L->stat[w][1] = ls; }
      asm volatile("s_waitcnt lgkmcnt(0)" ::: "memory");
      if (lane == 0) __hip_atomic_fetch_add(&L->cnt_stat, 1u, RLX_WG);
      PIN();
    }
    float acc[8];
#pragma unroll
    for (int k = 0; k < 8; ++k) acc[k] = 0.f;
#define V_STEP(i, REFILL) { \
      if (((i) & 3) == 0) p4 = *(LAS f32x4*)&L->pbuf[w][t * 64 + hf * 32 + (i)]; \
      const v4u vv = a[i]; const float p = p4[(i) & 3]; \
      acc[0] = fmaf(p, bf_lo(vv.x), acc[0]); acc[1] = fmaf(p, bf_hi(vv.x), acc[1]); \
      acc[2] = fmaf(p, bf_lo(vv.y), acc[2]); acc[3] = fmaf(p, bf_hi(vv.y), acc[3]); \
      acc[4] = fmaf(p, bf_lo(vv.z), acc[4]); acc[5] = fmaf(p, bf_hi(vv.z), acc[5]); \
      acc[6] = fmaf(p, bf_lo(vv.w), acc[6]); acc[7] = fmaf(p, bf_hi(vv.w), acc[7]); \
      if (REFILL) { _Pragma("unroll") for (int k = 0; k < 8; ++k) asm volatile("" : "+v"(acc[k])); \
        a[i] = ld16nt(rv, voff, (t + 1) * (RING * 4096) + (i) * 4096); PIN(); } }
    for (int t = 0; t < NIT - 1; ++t) {
      f32x4 p4;
#pragma unroll
      for (int i = 0; i < RING; ++i) V_STEP(i, true)
    }
    {
      const int t = NIT - 1;
      f32x4 p4;
#pragma unroll
      for (int i = 0; i < RING; ++i) V_STEP(i, false)
    }
#undef V_STEP
    {
      f32x4 o;
      o.x = swap32_add(acc[0], acc[4]); o.y = swap32_add(acc[1], acc[5]); o.z = swap32_add(acc[2], acc[6]); o.w = swap32_add(acc[3], acc[7]);
      *(LAS f32x4*)&L->ow[w][8 * c + 4 * hf] = o;
    }
    wg_barrier();
    return;
  }

  const int hw = wave - NSTREAM;
  const int lc = lane < 56 ? lane : 55;
  GAS u64* epoch_p = (GAS u64*)(ws + WS_EPOCH + (size_t)b * 256);
  const u64 epoch_v = __hip_atomic_load(epoch_p, RLX_AGENT);
  {
    const rsrc_t rq = mk_rsrc(wq, DM * NH * DH * 2);
    const int rg = 2 * hw + hf;
    const int qoff = (28 * rg * (NH * DH) + h * DH + 8 * c) * 2;
    v4u wr[28];
#pragma unroll
    for (int i = 0; i < 28; ++i) wr[i] = ld16(rq, qoff, i * (NH * DH * 2));
    v2u xp[7];
#pragma unroll
    for (int i = 0; i < 7; ++i) xp[i] = *(const GAS v2u*)(x + b * (DM / 2) + 14 * rg + 2 * i);
    if (hw == 0) {
      if (lane == 0) { L->cnt_stat = 0; L->cnt_help = 0; }
      if (lane < 56) {
        const v4u xv = *(const GAS v4u*)(x + b * (DM / 2) + 4 * lane);
        f32x4 lo = {bf_lo(xv.x), bf_hi(xv.x), bf_lo(xv.y), bf_hi(xv.y)}, hi = {bf_lo(xv.z), bf_hi(xv.z), bf_lo(xv.w), bf_hi(xv.w)};
        *(LAS f32x4*)&L->xs[8 * lane] = lo; *(LAS f32x4*)&L->xs[8 * lane + 4] = hi;
      }
    }
    PIN();
    float acc[8];
#pragma unroll
    for (int k = 0; k < 8; ++k) acc[k] = 0.f;
#pragma unroll
    for (int i = 0; i < 14; ++i) {
      const u32 xv = xp[i >> 1][i & 1];
#pragma unroll
      for (int k = 0; k < 4; ++k) {
        acc[2 * k] = dot2(pair_lo(wr[2 * i][k], wr[2 * i + 1][k]), xv, acc[2 * k]);
        acc[2 * k + 1] = dot2(pair_hi(wr[2 * i][k], wr[2 * i + 1][k]), xv, acc[2 * k + 1]);
      }
    }
    f32x4 o;
    o.x = swap32_add(acc[0], acc[4]); o.y = swap32_add(acc[1], acc[5]); o.z = swap32_add(acc[2], acc[6]); o.w = swap32_add(acc[3], acc[7]);
    *(LAS f32x4*)&L->qpart[hw][8 * c + 4 * hf] = o;
  }
  wg_barrier();
  const u64 epoch = (((u64)(u32)__builtin_amdgcn_readfirstlane((u32)(epoch_v >> 32)) << 32) | (u64)(u32)__builtin_amdgcn_readfirstlane((u32)epoch_v)) + 1;
  const u64 hash = epoch * 0x9E3779B97F4A7C15ull;
  const u64 tag40 = hash >> 24;
  const u32 tag32 = (u32)(hash >> 32);

  float g;
  {
    const v4u xv = *(const GAS v4u*)(x + b * (DM / 2) + 4 * lc);
    const v4u gv = *(const GAS v4u*)(gw + 4 * lc);
    float d = dot2(xv.x, gv.x, 0.f); d = dot2(xv.y, gv.y, d); d = dot2(xv.z, gv.z, d); d = dot2(xv.w, gv.w, d);
    d = lane < 56 ? d : 0.f;
    g = sigmoidf(sum64(d));
  }
  float yacc[8];
  {
    const rsrc_t rg_ = mk_rsrc(wg, DM * FF * 2), ru_ = mk_rsrc(wu, DM * FF * 2), rd_ = mk_rsrc(wd, FF * DM * 2);
    const int rs = lane >> 3, cc = (lane & 7) < 7 ? (lane & 7) : 6;
    const int foff = ((56 * hw + rs) * FF + UPW * j + 8 * cc) * 2;
    v4u gq[7], uq[7], dq[7];
#pragma unroll
    for (int i = 0; i < 7; ++i) { gq[i] = ld16(rg_, foff, i * 8 * FF * 2); uq[i] = ld16(ru_, foff, i * 8 * FF * 2); }
#pragma unroll
    for (int i = 0; i < 7; ++i) dq[i] = ld16(rd_, (UPW * j + 7 * hw) * (DM * 2) + lc * 16, i * (DM * 2));
    PIN();
    float v[16];
#pragma unroll
    for (int k = 0; k < 16; ++k) v[k] = 0.f;
#pragma unroll
    for (int i = 0; i < 7; ++i) {
      const float xv = L->xs[56 * hw + rs + 8 * i];
#pragma unroll
      for (int k = 0; k < 4; ++k) {
        v[2 * k] = fmaf(xv, bf_lo(gq[i][k]), v[2 * k]); v[2 * k + 1] = fmaf(xv, bf_hi(gq[i][k]), v[2 * k + 1]);
        v[8 + 2 * k] = fmaf(xv, bf_lo(uq[i][k]), v[8 + 2 * k]); v[8 + 2 * k + 1] = fmaf(xv, bf_hi(uq[i][k]), v[8 + 2 * k + 1]);
      }
    }
#pragma unroll
    for (int k = 0; k < 8; ++k) v[k] = swap32_add(v[k], v[k + 8]);
#pragma unroll
    for (int k = 0; k < 4; ++k) v[k] = swap16_add(v[k], v[k + 4]);
#pragma unroll
    for (int k = 0; k < 2; ++k) {
      const float t1 = v[k] + dpp<XOR8>(v[k]), t2 = v[k + 2] + dpp<XOR8>(v[k + 2]);
      v[k] = (lane & 8) ? t2 : t1;
    }
    if ((lane & 7) < 7) {
      const int e = ((lane >> 4) & 1) * 4 + ((lane >> 3) & 1) * 2;
      *(LAS f32x2*)&L->fpart[hw][hf * UPW + 8 * (lane & 7) + e] = f32x2{v[0], v[1]};
    }
    help_barrier(&L->cnt_help, NHELP, lane);
    float s = 0.f;
    {
      const int u = (lane & 7) < 7 ? (lane & 7) : 6, which = (lane >> 3) & 1;
#pragma unroll
      for (int k = 0; k < NHELP; ++k) s += L->fpart[k][which * UPW + 7 * hw + u];
    }
    const float up = dpp<XOR8>(s);
    const float hv = (1.0f - g) * (s * sigmoidf(s)) * up;
#pragma unroll
    for (int k = 0; k < 8; ++k) yacc[k] = 0.f;
#pragma unroll
    for (int i = 0; i < 7; ++i) {
      const float hs = rdlane(hv, i);
#pragma unroll
      for (int k = 0; k < 4; ++k) {
        yacc[2 * k] = fmaf(hs, bf_lo(dq[i][k]), yacc[2 * k]); yacc[2 * k + 1] = fmaf(hs, bf_hi(dq[i][k]), yacc[2 * k + 1]);
      }
    }
#pragma unroll
    for (int k = 0; k < 8; ++k) asm volatile("" : "+v"(yacc[k]));
    asm volatile("" : "+v"(g));
    PIN();
  }
  u32 wp[16][8];
  {
    const rsrc_t ro = mk_rsrc(wo, NH * DH * DM * 2);
    const int ooff = (h * DH + 32 * hw) * (DM * 2) + lc * 16;
#pragma unroll
    for (int part = 0; part < 4; ++part) {
      v4u r[8];
#pragma unroll
      for (int i = 0; i < 8; ++i) r[i] = ld16(ro, ooff, (part * 8 + i) * (DM * 2));
      PIN();
#pragma unroll
      for (int i = 0; i < 4; ++i)
#pragma unroll
        for (int k = 0; k < 4; ++k) {
          wp[part * 4 + i][2 * k] = pair_lo(r[2 * i][k], r[2 * i + 1][k]);
          wp[part * 4 + i][2 * k + 1] = pair_hi(r[2 * i][k], r[2 * i + 1][k]);
        }
#pragma unroll
      for (int i = 0; i < 4; ++i)
#pragma unroll
        for (int k = 0; k < 8; ++k) asm volatile("" : "+v"(wp[part * 4 + i][k]));
      PIN();
    }
  }
  if (hw == 0) {
    for (u32 spins = 0; spins < SPIN_CAP; ++spins) {
      if (__builtin_amdgcn_readfirstlane(lds_ld(&L->cnt_stat)) >= NSTREAM) break;
      __builtin_amdgcn_s_sleep(16);
    }
    asm volatile("" ::: "memory");
    const float mw = L->stat[lane & 3][0], lw = L->stat[lane & 3][1];
    float ms = fmaxf(mw, dpp<XOR1>(mw)); ms = fmaxf(ms, dpp<XOR2>(ms));
    float ls = lw * __builtin_amdgcn_exp2f(mw - ms); ls += dpp<XOR1>(ls); ls += dpp<XOR2>(ls);
    GAS u64* mine = (GAS u64*)(ws + WS_STAT + (size_t)blk * 128);
    GAS u64* other = (GAS u64*)(ws + WS_STAT + (size_t)(blk ^ 1) * 128);
    if (lane < 2) __hip_atomic_store(mine + lane, ((u64)tag32 << 32) | __builtin_bit_cast(u32, lane == 0 ? ms : ls), RLX_AGENT);
    u64 got = 0;
    for (u32 spins = 0; spins < SPIN_CAP; ++spins) {
      got = __hip_atomic_load(other + (lane & 1), RLX_AGENT);
      if (__all((u32)(got >> 32) == tag32)) break;
      __builtin_amdgcn_s_sleep(16);
    }
    const float ov = __builtin_bit_cast(float, (u32)got);
    const float mo = rdlane(ov, 0), lo = rdlane(ov, 1);
    const float M = fmaxf(ms, mo);
    const float Lsum = ls * __builtin_amdgcn_exp2f(ms - M) + lo * __builtin_amdgcn_exp2f(mo - M);
    if (lane < 4) L->fac[lane] = g * __builtin_amdgcn_exp2f(mw - M) / Lsum;
  }
  wg_barrier();
  {
    const int d = 32 * hw + c;
    float v = L->fac[2 * hf] * L->ow[2 * hf][d] + L->fac[2 * hf + 1] * L->ow[2 * hf + 1][d];
    v = swap32_add(v, v);
    const u32 pk = cvtpk(v, dpp<XOR1>(v));
#pragma unroll
    for (int i = 0; i < 16; ++i) {
      const u32 op = __builtin_amdgcn_readlane(pk, 2 * i);
#pragma unroll
      for (int k = 0; k < 8; ++k) yacc[k] = dot2(wp[i][k], op, yacc[k]);
    }
    if (lane < 56) {
      *(LAS f32x4*)&L->yw[hw][8 * lane] = f32x4{yacc[0], yacc[1], yacc[2], yacc[3]};
      *(LAS f32x4*)&L->yw[hw][8 * lane + 4] = f32x4{yacc[4], yacc[5], yacc[6], yacc[7]};
    }
  }
  wg_barrier();
  if (hw == 7) return;
  {
    const int t = hw * 64 + lane;
    float s = 0.f;
#pragma unroll
    for (int k = 0; k < NHELP; ++k) s += L->yw[k][t];
    const u32 bits = __builtin_bit_cast(u32, s) + 0x80u;
    GAS u64* ys = (GAS u64*)(ws + WS_Y);
    __hip_atomic_store(ys + (size_t)blk * DM + t, (tag40 << 24) | (u64)(bits >> 8), RLX_AGENT);
    const int col = CPW * j + 2 * hw + hf;
    GAS u64* src = ys + (size_t)(b * GRP + c) * DM + col;
    u64 got = 0;
    for (u32 spins = 0; spins < SPIN_CAP; ++spins) {
      got = __hip_atomic_load(src, RLX_AGENT);
      if (__all((got >> 24) == tag40)) break;
      __builtin_amdgcn_s_sleep(1);
    }
    const float tot = sum32(__builtin_bit_cast(float, (u32)(got << 8)));
    if (c == 0) out[b * DM + col] = (unsigned short)(cvtpk(tot, tot) & 0xffffu);
    if (j == 0 && hw == 0 && lane == 0) __hip_atomic_store(epoch_p, epoch, RLX_AGENT);
  }
}

}

extern "C" void kernel_launch(void* const* d_in, const int* in_sizes, int n_in, void* d_out, int out_size, void* d_ws, size_t ws_size,
                              hipStream_t stream) {
  (void)in_sizes; (void)n_in; (void)out_size; (void)ws_size;
  hipLaunchKernelGGL(fused_decode, dim3(NWG), dim3(NTHR), 0, stream,
                     (const u32*)d_in[0], (const u32*)d_in[1], (const u32*)d_in[2], (const u32*)d_in[3], (const u32*)d_in[4],
                     (const u32*)d_in[6], (const u32*)d_in[7], (const u32*)d_in[8], (const u32*)d_in[9],
                     (unsigned short*)d_out, (unsigned char*)d_ws);
}
```

```cpp
#include <hip/hip_runtime.h>
#include <hip/hip_bf16.h>
#include <stdint.h>

namespace {

constexpr int NB = 8, DM = 448, NH = 16, DH = 256, FF = 1792;
constexpr int NWG = 256;
constexpr int NS = 8, NHELP = 4, NWAVE = NS + NHELP, NTHR = NWAVE * 64;
constexpr int RING = 32;
constexpr int ROWB = NS * 1024;
constexpr int CHUNKB = 2048 * 512;
constexpr int NIT = CHUNKB / ROWB / RING;
constexpr int GRP = 32;
constexpr int UPW = 64;
constexpr int CPW = DM / GRP;
constexpr float SC2 = 0.0625f * 1.4426950408889634f;
constexpr unsigned SPIN_CAP = 1u << 15;
constexpr int IFC = 4;
constexpr int HIFC = 8;

constexpr size_t WS_EPOCH = 0;
constexpr size_t WS_STAT = 4096;
constexpr size_t WS_Q = WS_STAT + (size_t)NWG * 128;
constexpr size_t WS_Y = WS_Q + (size_t)NH * 4 * NB * DH * 8;
constexpr size_t WS_END = WS_Y + (size_t)NWG * DM * 8;
static_assert(WS_END <= ((size_t)256 << 20), "workspace");

typedef unsigned u32;
typedef unsigned long long u64;
typedef u32 v4u __attribute__((ext_vector_type(4)));
typedef float f32x4 __attribute__((ext_vector_type(4)));
typedef float f32x2 __attribute__((ext_vector_type(2)));
typedef __bf16 bf16x2 __attribute__((ext_vector_type(2)));
typedef short bf16x8 __attribute__((ext_vector_type(8)));
#define GAS __attribute__((address_space(1)))
#define LAS __attribute__((address_space(3)))
#define RLX_AGENT __ATOMIC_RELAXED, __HIP_MEMORY_SCOPE_AGENT
#define RLX_WG __ATOMIC_RELAXED, __HIP_MEMORY_SCOPE_WORKGROUP
typedef __amdgpu_buffer_rsrc_t rsrc_t;

struct __attribute__((aligned(16))) Lds {
  v4u wo_hi[NS][8][2][56];
  float ptab[33][64];
  float fpart[NHELP][128];
  float ow[NS][DH];
  float yw[NWAVE][DM];
  float xs[DM];
  unsigned short q[DH];
  float stat[NS][2];
  float fac[NS];
  u64 tag;
  float gate;
  u32 cnt_q;
  u32 cnt_stat;
  u32 cnt_help;
  u32 cnt_wo;
};
static_assert(sizeof(Lds) <= 163840, "LDS");
static_assert(IFC <= 4, "the last turn waits for the last four rows only");

__device__ __forceinline__ rsrc_t mk_rsrc(const void* p, u32 bytes) {
  return __builtin_amdgcn_make_buffer_rsrc(const_cast<void*>(p), 0, (int)bytes, 0x00020000);
}
template <int N> __device__ __forceinline__ void pace() { asm volatile("s_waitcnt vmcnt(%0)" :: "n"(N - 1) : "memory"); }
__device__ __forceinline__ v4u ld16(rsrc_t r, int voff, int soff) { return __builtin_amdgcn_raw_buffer_load_b128(r, voff, soff, 0); }
__device__ __forceinline__ v4u ld16p(rsrc_t r, int voff, int soff) { pace<HIFC>(); return __builtin_amdgcn_raw_buffer_load_b128(r, voff, soff, 0); }
__device__ __forceinline__ float dot2(u32 a, u32 b, float acc) {
  return __builtin_amdgcn_fdot2_f32_bf16(__builtin_bit_cast(bf16x2, a), __builtin_bit_cast(bf16x2, b), acc, false);
}
__device__ __forceinline__ float bf_lo(u32 v) { return __builtin_bit_cast(float, v << 16); }
__device__ __forceinline__ float bf_hi(u32 v) { return __builtin_bit_cast(float, v & 0xffff0000u); }
__device__ __forceinline__ u32 cvtpk(float lo, float hi) {
  const bf16x2 r = {(__bf16)lo, (__bf16)hi};
  return __builtin_bit_cast(u32, r);
}
__device__ __forceinline__ u32 pair_lo(u32 r0, u32 r1) { return __builtin_amdgcn_perm(r1, r0, 0x05040100u); }
__device__ __forceinline__ u32 pair_hi(u32 r0, u32 r1) { return __builtin_amdgcn_perm(r1, r0, 0x07060302u); }
template <int CTRL> __device__ __forceinline__ float dpp(float x) {
  return __builtin_bit_cast(float, __builtin_amdgcn_mov_dpp(__builtin_bit_cast(int, x), CTRL, 0xf, 0xf, true));
}
constexpr int XOR1 = 0xB1, XOR2 = 0x4E, XOR7 = 0x141, XOR8 = 0x128;
__device__ __forceinline__ float swap16_add(float a, float b) {
  auto s = __builtin_amdgcn_permlane16_swap(__float_as_uint(a), __float_as_uint(b), false, false);
  return __uint_as_float(s[0]) + __uint_as_float(s[1]);
}
__device__ __forceinline__ float swap32_add(float a, float b) {
  auto s = __builtin_amdgcn_permlane32_swap(__float_as_uint(a), __float_as_uint(b), false, false);
  return __uint_as_float(s[0]) + __uint_as_float(s[1]);
}
__device__ __forceinline__ float swap16_max(float a) {
  auto s = __builtin_amdgcn_permlane16_swap(__float_as_uint(a), __float_as_uint(a), false, false);
  return fmaxf(__uint_as_float(s[0]), __uint_as_float(s[1]));
}
__device__ __forceinline__ float swap32_max(float a) {
  auto s = __builtin_amdgcn_permlane32_swap(__float_as_uint(a), __float_as_uint(a), false, false);
  return fmaxf(__uint_as_float(s[0]), __uint_as_float(s[1]));
}
__device__ __forceinline__ float sum32(float v) {
  v += dpp<XOR1>(v); v += dpp<XOR2>(v); v += dpp<XOR7>(v); v += dpp<XOR8>(v);
  return swap16_add(v, v);
}
__device__ __forceinline__ float sum64(float v) { v = sum32(v); return swap32_add(v, v); }
__device__ __forceinline__ float max64(float v) {
  v = fmaxf(v, dpp<XOR1>(v)); v = fmaxf(v, dpp<XOR2>(v)); v = fmaxf(v, dpp<XOR7>(v)); v = fmaxf(v, dpp<XOR8>(v));
  return swap32_max(swap16_max(v));
}
__device__ __forceinline__ u32 rfl(u32 v) { return (u32)__builtin_amdgcn_readfirstlane((int)v); }
__device__ __forceinline__ float rdlane(float v, int l) { return __builtin_bit_cast(float, __builtin_amdgcn_readlane(__builtin_bit_cast(int, v), l)); }
__device__ __forceinline__ void wg_barrier() { asm volatile("s_waitcnt lgkmcnt(0)\n\ts_barrier" ::: "memory"); }
__device__ __forceinline__ void lds_drain() { asm volatile("s_waitcnt lgkmcnt(0)" ::: "memory"); }
__device__ __forceinline__ u32 lds_ld(LAS u32* p) { return __hip_atomic_load(p, RLX_WG); }
__device__ __forceinline__ float sigmoidf(float z) { return 1.0f / (1.0f + __expf(-z)); }
__device__ __forceinline__ u64 uni64(u64 v) { return ((u64)rfl((u32)(v >> 32)) << 32) | (u64)rfl((u32)v); }
__device__ __forceinline__ u64 pack24(u64 tag, float v) { return (tag & ~0xffffffull) | (u64)((__builtin_bit_cast(u32, v) + 0x80u) >> 8); }
__device__ __forceinline__ bool ok24(u64 tag, u64 g) { return ((g ^ tag) >> 24) == 0; }
__device__ __forceinline__ float val24(u64 g) { return __builtin_bit_cast(float, (u32)(g << 8)); }
template <int SLEEP> __device__ __forceinline__ void lds_wait_ge(LAS u32* p, u32 target) {
  for (u32 spins = 0; spins < SPIN_CAP; ++spins) {
    if (rfl(lds_ld(p)) >= target) break;
    __builtin_amdgcn_s_sleep(SLEEP);
  }
  asm volatile("" ::: "memory");
}
__device__ __forceinline__ void lds_spin_ge(u32 addr, u32 target) {
  u32 t, v, cnt;
  asm volatile("s_mov_b32 %2, 0x40000\n"
               "1:\n\tds_read_b32 %0, %3\n\ts_waitcnt lgkmcnt(0)\n\tv_readfirstlane_b32 %1, %0\n\ts_cmp_ge_u32 %1, %4\n\ts_cbranch_scc1 2f\n\t"
               "s_sub_u32 %2, %2, 1\n\ts_cbranch_scc1 2f\n\ts_sleep 1\n\ts_branch 1b\n2:"
               : "=&v"(t), "=&s"(v), "=&s"(cnt) : "v"(addr), "s"(target) : "scc", "memory");
}
__device__ __forceinline__ void ring_use(v4u& slot) { asm volatile("" : "+v"(slot)); }
template <int N> __device__ __forceinline__ void ring_load_first(v4u& slot, int voff, rsrc_t r, int soff) {
  asm volatile("s_waitcnt vmcnt(%4)\n\tbuffer_load_dwordx4 %0, %1, %2, %3 offen nt" : "=v"(slot) : "v"(voff), "s"(r), "s"(soff), "n"(N - 1) : "memory");
}
__device__ __forceinline__ void ring_load4(v4u& s0, v4u& s1, v4u& s2, v4u& s3, int voff, rsrc_t r, int soff, int t, u32 lim) {
  int o1, o2, o3;
  asm volatile("s_add_u32 %4, %9, %12\n\ts_add_u32 %5, %9, 2*%12\n\ts_add_u32 %6, %9, 3*%12\n\ts_cmp_eq_u32 %10, %11\n\ts_cbranch_scc1 2f\n\t"
               "s_cmp_eq_u32 %13, 3\n\ts_cbranch_scc1 1f\n\t"
               "s_waitcnt vmcnt(3)\n\tbuffer_load_dwordx4 %0, %7, %8, %9 offen nt\n\t"
               "s_waitcnt vmcnt(3)\n\tbuffer_load_dwordx4 %1, %7, %8, %4 offen nt\n\t"
               "s_waitcnt vmcnt(3)\n\tbuffer_load_dwordx4 %2, %7, %8, %5 offen nt\n\t"
               "s_waitcnt vmcnt(3)\n\tbuffer_load_dwordx4 %3, %7, %8, %6 offen nt\n\ts_branch 2f\n"
               "1:\n\ts_waitcnt vmcnt(2)\n\tbuffer_load_dwordx4 %0, %7, %8, %9 offen nt\n\t"
               "s_waitcnt vmcnt(2)\n\tbuffer_load_dwordx4 %1, %7, %8, %4 offen nt\n\t"
               "s_waitcnt vmcnt(2)\n\tbuffer_load_dwordx4 %2, %7, %8, %5 offen nt\n\t"
               "s_waitcnt vmcnt(2)\n\tbuffer_load_dwordx4 %3, %7, %8, %6 offen nt\n2:"
               : "+v"(s0), "+v"(s1), "+v"(s2), "+v"(s3), "=&s"(o1), "=&s"(o2), "=&s"(o3)
               : "v"(voff), "s"(r), "s"(soff), "s"(t), "n"(NIT - 1), "n"(ROWB), "s"(lim) : "scc", "memory");
}
template <int N> __device__ __forceinline__ void drain_if_last(int t) {
  asm volatile("s_cmp_lg_u32 %0, %2\n\ts_cbranch_scc1 1f\n\ts_waitcnt vmcnt(%1)\n1:" :: "s"(t), "n"(N), "n"(NIT - 1) : "scc", "memory");
}
__device__ __forceinline__ void help_barrier(LAS u32* cnt, u32 target, int lane) {
  lds_drain();
  if (lane == 0) __hip_atomic_fetch_add(cnt, 1u, RLX_WG);
  lds_wait_ge<1>(cnt, target);
}

#define PIN() __builtin_amdgcn_sched_barrier(0)

__global__ void __launch_bounds__(NTHR, 3)
fused_decode(const u32* __restrict__ x, const u32* __restrict__ kc, const u32* __restrict__ vc, const u32* __restrict__ wq,
             const u32* __restrict__ wo, const u32* __restrict__ wg, const u32* __restrict__ wu, const u32* __restrict__ wd,
             const u32* __restrict__ gw, unsigned short* __restrict__ out, unsigned char* __restrict__ ws) {
  __shared__ Lds lds_;
  LAS Lds* L = (LAS Lds*)&lds_;
  const int tid = threadIdx.x, lane = tid & 63, wave = __builtin_amdgcn_readfirstlane(tid >> 6);
  const int blk = blockIdx.x, r8 = blk & 7;
  const int b = blk >> 5, h = 4 * ((blk >> 3) & 3) + ((0x33012210u >> (4 * r8)) & 3), s = (0xB8 >> r8) & 1;
  const int chunk = (b * NH + h) * 2 + s, j = chunk & 31;
  const int hf = lane >> 5, c = lane & 31;
  float yacc[8];
  u32 wp[16][8];

  if (__builtin_expect(wave < NS, 1)) {
    const int w = wave;
    const rsrc_t rk = mk_rsrc((const char*)kc + (size_t)chunk * CHUNKB, CHUNKB);
    const rsrc_t rv = mk_rsrc((const char*)vc + (size_t)chunk * CHUNKB, CHUNKB);
    const int voff = lane * 16 + w * 1024;
    const u32 lim = rfl(((unsigned)__builtin_amdgcn_s_getreg(20 | (3 << 11)) & 1u) ? 4u : 3u);
    v4u a[RING];
#pragma unroll
    for (int i = 0; i < RING; ++i) {
      if (i == IFC) wg_barrier();
      ring_load_first<IFC>(a[i], voff, rk, i * ROWB);
    }
    lds_spin_ge((u32)(uintptr_t)L + offsetof(Lds, cnt_q), NHELP);
    const v4u q = *(LAS v4u*)&L->q[8 * c];
    const int sidx = (c >> 1) + 16 * (c & 1);
#pragma clang loop unroll(disable)
    for (int t = 0; t < NIT; ++t) {
      asm volatile("" : "+s"(t));
      const bool last = t == NIT - 1;
      const rsrc_t rn = last ? rv : rk;
      const int nb = last ? 0 : (t + 1) * (RING * ROWB);
      float part[2];
#pragma unroll
      for (int g2 = 0; g2 < 2; ++g2) {
        float xx[16];
#pragma unroll
        for (int i4 = 0; i4 < 16; i4 += 4) {
          const int ii = g2 * 16 + i4;
#pragma unroll
          for (int i = 0; i < 4; ++i) {
            ring_use(a[ii + i]);
            const v4u kk = a[ii + i];
            float d = dot2(kk.x, q.x, 0.f);
            d = dot2(kk.y, q.y, d); d = dot2(kk.z, q.z, d); d = dot2(kk.w, q.w, d);
            xx[i4 + i] = d;
          }
#pragma unroll
          for (int i = 0; i < 4; ++i) asm volatile("" : "+v"(xx[i4 + i]));
          ring_load4(a[ii], a[ii + 1], a[ii + 2], a[ii + 3], voff, rn, nb + ii * ROWB, 0, lim);
          PIN();
        }
#pragma unroll
        for (int k = 0; k < 8; ++k) xx[k] = swap16_add(xx[k], xx[k + 8]);
#define TR_STEP(HALF, CTRL, BIT) _Pragma("unroll") for (int k = 0; k < (HALF); ++k) { \
          const float t1 = xx[k] + dpp<CTRL>(xx[k]), t2 = xx[k + (HALF)] + dpp<CTRL>(xx[k + (HALF)]); xx[k] = (c & (BIT)) ? t2 : t1; }
        TR_STEP(4, XOR8, 8) TR_STEP(2, XOR7, 4) TR_STEP(1, XOR2, 2)
#undef TR_STEP
        part[g2] = xx[0];
        PIN();
      }
      const float t1 = part[0] + dpp<XOR1>(part[0]), t2 = part[1] + dpp<XOR1>(part[1]);
      const float sc = (c & 1) ? t2 : t1;
      L->ptab[8 * t + (sidx >> 2)][8 * w + 4 * hf + (sidx & 3)] = bf_lo(cvtpk(sc, sc));
      PIN();
    }
    {
      float sc[NIT];
      LAS float* pp = &L->ptab[sidx >> 2][8 * w + 4 * hf + (sidx & 3)];
#pragma unroll
      for (int t = 0; t < NIT; ++t) sc[t] = pp[t * 8 * 64];
      const float m2 = max64(fmaxf(fmaxf(sc[0], sc[1]), fmaxf(sc[2], sc[3]))) * SC2;
      float ls = 0.f;
#pragma unroll
      for (int t = 0; t < NIT; ++t) {
        const float p = __builtin_amdgcn_exp2f(fmaf(sc[t], SC2, -m2));
        pp[t * 8 * 64] = p;
        ls += p;
      }
      ls = sum64(ls);
      if (lane == 0) { L->stat[w][0] = m2; L->stat[w][1] = ls; }
      lds_drain();
      if (lane == 0) __hip_atomic_fetch_add(&L->cnt_stat, 1u, RLX_WG);
      PIN();
    }
    float acc[8];
#pragma unroll
    for (int k = 0; k < 8; ++k) acc[k] = 0.f;
    {
      LAS float* pt = &L->ptab[0][8 * w + 4 * hf];
      f32x4 pn = *(LAS f32x4*)pt;
#pragma clang loop unroll(disable)
      for (int t = 0; t < NIT; ++t) {
        asm volatile("" : "+s"(t));
        const int nb = (t + 1) * (RING * ROWB);
#pragma unroll
        for (int u = 0; u < 8; ++u) {
          const f32x4 p4 = pn;
          pn = *(LAS f32x4*)&pt[(8 * t + u + 1) * 64];
          if (u == 7) drain_if_last<0>(t);
          PIN();
#pragma unroll
          for (int i = 0; i < 4; ++i) {
            ring_use(a[4 * u + i]);
            const v4u vv = a[4 * u + i];
            const float p = p4[i];
            acc[0] = fmaf(p, bf_lo(vv.x), acc[0]); acc[1] = fmaf(p, bf_hi(vv.x), acc[1]);
            acc[2] = fmaf(p, bf_lo(vv.y), acc[2]); acc[3] = fmaf(p, bf_hi(vv.y), acc[3]);
            acc[4] = fmaf(p, bf_lo(vv.z), acc[4]); acc[5] = fmaf(p, bf_hi(vv.z), acc[5]);
            acc[6] = fmaf(p, bf_lo(vv.w), acc[6]); acc[7] = fmaf(p, bf_hi(vv.w), acc[7]);
          }
#pragma unroll
          for (int e = 0; e < 8; ++e) asm volatile("" : "+v"(acc[e]));
          ring_load4(a[4 * u], a[4 * u + 1], a[4 * u + 2], a[4 * u + 3], voff, rv, nb + 4 * u * ROWB, t, (u & 1) ? 4u : lim);
          PIN();
        }
      }
    }
    lds_spin_ge((u32)(uintptr_t)L + offsetof(Lds, cnt_wo), NHELP);
#pragma unroll
    for (int i = 0; i < 8; ++i) {
      const v4u lo = L->wo_hi[w][i][0][lane < 56 ? lane : 55], hi = L->wo_hi[w][i][1][lane < 56 ? lane : 55];
      wp[i][0] = lo.x; wp[i][1] = lo.y; wp[i][2] = lo.z; wp[i][3] = lo.w; wp[i][4] = hi.x; wp[i][5] = hi.y; wp[i][6] = hi.z; wp[i][7] = hi.w;
    }
#pragma unroll
    for (int k = 0; k < 8; ++k) yacc[k] = 0.f;
    {
      f32x4 o;
      o.x = swap32_add(acc[0], acc[4]); o.y = swap32_add(acc[1], acc[5]); o.z = swap32_add(acc[2], acc[6]); o.w = swap32_add(acc[3], acc[7]);
      *(LAS f32x4*)&L->ow[w][8 * c + 4 * hf] = o;
    }
  } else {
  const int hw = wave - NS;
  if (hw == 0 && lane == 0) { L->cnt_q = 0; L->cnt_stat = 0; L->cnt_help = 0; L->cnt_wo = 0; }
  wg_barrier();
  __builtin_amdgcn_s_setprio(3);
  const int lc = lane < 56 ? lane : 55;
  const u64 epoch_v = __hip_atomic_load((GAS u64*)(ws + WS_EPOCH), RLX_AGENT);
  u64 tag;
  float g;
  {
    const int cb = 2 * s + (b >> 2), kq = b & 3;
    const rsrc_t rq = mk_rsrc(wq, DM * NH * DH * 2), rx = mk_rsrc(x, NB * DM * 2);
    const int qoff = ((128 * kq + 8 * (lane >> 4)) * (NH * DH) + h * DH + 64 * cb + 16 * hw + (lane & 15)) * 2;
    u32 wv[4][8];
    v4u xa[4];
#pragma unroll
    for (int kk = 0; kk < 4; ++kk) {
#pragma unroll
      for (int jj = 0; jj < 8; ++jj) wv[kk][jj] = __builtin_amdgcn_raw_buffer_load_b16(rq, qoff, (kk * 32 + jj) * (NH * DH * 2), 0);
      const int k0 = 128 * kq + 32 * kk;
      xa[kk] = ld16(rx, k0 < DM ? ((lane & 7) * DM + k0 + 8 * (lane >> 4)) * 2 : 0x7fffff00, 0);
    }
    const v4u xv = ld16(rx, (b * DM + 8 * lc) * 2, 0);
    const v4u gv = ld16(mk_rsrc(gw, DM * 2), lc * 16, 0);
    PIN();
    f32x4 acc = {0.f, 0.f, 0.f, 0.f};
#pragma unroll
    for (int kk = 0; kk < 4; ++kk) {
      const v4u bf = {wv[kk][0] | (wv[kk][1] << 16), wv[kk][2] | (wv[kk][3] << 16), wv[kk][4] | (wv[kk][5] << 16), wv[kk][6] | (wv[kk][7] << 16)};
      acc = __builtin_amdgcn_mfma_f32_16x16x32_bf16(__builtin_bit_cast(bf16x8, xa[kk]), __builtin_bit_cast(bf16x8, bf), acc, 0, 0, 0);
    }
    tag = uni64((uni64(epoch_v) + 1) * 0x9E3779B97F4A7C15ull);
    GAS u64* qs = (GAS u64*)(ws + WS_Q) + (size_t)h * (4 * NB * DH);
    if (lane < 32) {
#pragma unroll
      for (int r = 0; r < 4; ++r)
        __hip_atomic_store(qs + (size_t)(kq * NB + 4 * (lane >> 4) + r) * DH + 64 * cb + 16 * hw + (lane & 15), pack24(tag, acc[r]), RLX_AGENT);
    }
    if (hw == 0) {
      if (lane == 0) L->tag = tag;
      if (lane < 56) {
        *(LAS f32x4*)&L->xs[8 * lane] = f32x4{bf_lo(xv.x), bf_hi(xv.x), bf_lo(xv.y), bf_hi(xv.y)};
        *(LAS f32x4*)&L->xs[8 * lane + 4] = f32x4{bf_lo(xv.z), bf_hi(xv.z), bf_lo(xv.w), bf_hi(xv.w)};
      }
    }
    float d = dot2(xv.x, gv.x, 0.f); d = dot2(xv.y, gv.y, d); d = dot2(xv.z, gv.z, d); d = dot2(xv.w, gv.w, d);
    d = lane < 56 ? d : 0.f;
    g = sigmoidf(sum64(d));
    if (hw == 0 && lane == 0) L->gate = g;
    u64 got[4];
    for (u32 spins = 0; spins < SPIN_CAP; ++spins) {
      bool ok = true;
#pragma unroll
      for (int k = 0; k < 4; ++k) { got[k] = __hip_atomic_load(qs + (size_t)(k * NB + b) * DH + 64 * hw + lane, RLX_AGENT); ok &= ok24(tag, got[k]); }
      if (__all(ok)) break;
      __builtin_amdgcn_s_sleep(4);
    }
    const float qv = (val24(got[0]) + val24(got[1])) + (val24(got[2]) + val24(got[3]));
    L->q[64 * hw + lane] = (unsigned short)(cvtpk(qv, qv) & 0xffffu);
    lds_drain();
    if (lane == 0) __hip_atomic_fetch_add(&L->cnt_q, 1u, RLX_WG);
    __builtin_amdgcn_s_setprio(0);
    lds_wait_ge<1>(&L->cnt_q, NHELP);
  }
#pragma unroll
  for (int k = 0; k < 8; ++k) yacc[k] = 0.f;
  const int vslot = r8 * 4 + ((blk >> 3) & 3);
  if ((vslot & 7) != 7) {
    const int jf = vslot - (vslot >> 3);
    const rsrc_t rg_ = mk_rsrc(wg, DM * FF * 2), ru_ = mk_rsrc(wu, DM * FF * 2), rd_ = mk_rsrc(wd, FF * DM * 2);
    const int rs = lane >> 3;
    const int foff = ((112 * hw + rs) * FF + UPW * jf + 8 * (lane & 7)) * 2;
    float v[16];
#pragma unroll
    for (int k = 0; k < 16; ++k) v[k] = 0.f;
#pragma unroll
    for (int half = 0; half < 2; ++half) {
      v4u gq[7], uq[7];
#pragma unroll
      for (int i = 0; i < 7; ++i) { gq[i] = ld16p(rg_, foff, (half * 7 + i) * 8 * FF * 2); uq[i] = ld16p(ru_, foff, (half * 7 + i) * 8 * FF * 2); PIN(); }
#pragma unroll
      for (int i = 0; i < 7; ++i) {
        const float xv = L->xs[112 * hw + rs + 8 * (half * 7 + i)];
#pragma unroll
        for (int k = 0; k < 4; ++k) {
          v[2 * k] = fmaf(xv, bf_lo(gq[i][k]), v[2 * k]); v[2 * k + 1] = fmaf(xv, bf_hi(gq[i][k]), v[2 * k + 1]);
          v[8 + 2 * k] = fmaf(xv, bf_lo(uq[i][k]), v[8 + 2 * k]); v[8 + 2 * k + 1] = fmaf(xv, bf_hi(uq[i][k]), v[8 + 2 * k + 1]);
        }
      }
#pragma unroll
      for (int k = 0; k < 16; ++k) asm volatile("" : "+v"(v[k]));
      PIN();
    }
    v4u dq[16];
#pragma unroll
    for (int i = 0; i < 16; ++i) { dq[i] = ld16p(rd_, (UPW * jf + 16 * hw) * (DM * 2) + lc * 16, i * (DM * 2)); PIN(); }
#pragma unroll
    for (int k = 0; k < 8; ++k) v[k] = swap32_add(v[k], v[k + 8]);
#pragma unroll
    for (int k = 0; k < 4; ++k) v[k] = swap16_add(v[k], v[k + 4]);
#pragma unroll
    for (int k = 0; k < 2; ++k) {
      const float t1 = v[k] + dpp<XOR8>(v[k]), t2 = v[k + 2] + dpp<XOR8>(v[k + 2]);
      v[k] = (lane & 8) ? t2 : t1;
    }
    {
      const int e = ((lane >> 4) & 1) * 4 + ((lane >> 3) & 1) * 2;
      *(LAS f32x2*)&L->fpart[hw][hf * UPW + 8 * (lane & 7) + e] = f32x2{v[0], v[1]};
    }
    help_barrier(&L->cnt_help, NHELP, lane);
    float sm = 0.f;
#pragma unroll
    for (int k = 0; k < NHELP; ++k) sm += L->fpart[k][((lane >> 4) & 1) * UPW + 16 * hw + (lane & 15)];
    float up;
    { auto sw = __builtin_amdgcn_permlane16_swap(__float_as_uint(sm), __float_as_uint(sm), false, false); up = __uint_as_float(sw[1]); }
    const float hv = (1.0f - g) * (sm * sigmoidf(sm)) * up;
#pragma unroll
    for (int i = 0; i < 16; ++i) {
      const float hs = rdlane(hv, i);
#pragma unroll
      for (int k = 0; k < 4; ++k) {
        yacc[2 * k] = fmaf(hs, bf_lo(dq[i][k]), yacc[2 * k]); yacc[2 * k + 1] = fmaf(hs, bf_hi(dq[i][k]), yacc[2 * k + 1]);
      }
    }
  }
#pragma unroll
  for (int k = 0; k < 8; ++k) asm volatile("" : "+v"(yacc[k]));
  PIN();
  {
    const rsrc_t ro = mk_rsrc(wo, NH * DH * DM * 2);
    const int ooff = (h * DH + 32 * hw) * (DM * 2) + lc * 16;
#pragma unroll
    for (int part = 0; part < 4; ++part) {
      v4u r[8];
#pragma unroll
      for (int i = 0; i < 8; ++i) { r[i] = ld16p(ro, ooff, (128 + part * 8 + i) * (DM * 2)); PIN(); }
#pragma unroll
      for (int i = 0; i < 4; ++i) {
        const v4u lo = {pair_lo(r[2 * i][0], r[2 * i + 1][0]), pair_hi(r[2 * i][0], r[2 * i + 1][0]), pair_lo(r[2 * i][1], r[2 * i + 1][1]), pair_hi(r[2 * i][1], r[2 * i + 1][1])};
        const v4u hi = {pair_lo(r[2 * i][2], r[2 * i + 1][2]), pair_hi(r[2 * i][2], r[2 * i + 1][2]), pair_lo(r[2 * i][3], r[2 * i + 1][3]), pair_hi(r[2 * i][3], r[2 * i + 1][3])};
        if (lane < 56) { L->wo_hi[2 * hw + (part >> 1)][(part & 1) * 4 + i][0][lane] = lo; L->wo_hi[2 * hw + (part >> 1)][(part & 1) * 4 + i][1][lane] = hi; }
      }
      PIN();
    }
    lds_drain();
    if (lane == 0) __hip_atomic_fetch_add(&L->cnt_wo, 1u, RLX_WG);
#pragma unroll
    for (int part = 0; part < 8; ++part) {
      v4u r[4];
#pragma unroll
      for (int i = 0; i < 4; ++i) { r[i] = ld16p(ro, ooff, (part * 4 + i) * (DM * 2)); PIN(); }
#pragma unroll
      for (int i = 0; i < 2; ++i)
#pragma unroll
        for (int k = 0; k < 4; ++k) {
          wp[part * 2 + i][2 * k] = pair_lo(r[2 * i][k], r[2 * i + 1][k]);
          wp[part * 2 + i][2 * k + 1] = pair_hi(r[2 * i][k], r[2 * i + 1][k]);
        }
#pragma unroll
      for (int i = 0; i < 2; ++i)
#pragma unroll
        for (int k = 0; k < 8; ++k) asm volatile("" : "+v"(wp[part * 2 + i][k]));
      PIN();
    }
  }
  if (hw == 0) {
    lds_wait_ge<16>(&L->cnt_stat, NS);
    GAS u64* const slot = (GAS u64*)(ws + WS_STAT + (size_t)chunk * 128);
    GAS u64* const oslot = (GAS u64*)(ws + WS_STAT + (size_t)(chunk ^ 1) * 128);
    const u32 tag32 = (u32)(uni64(L->tag) >> 32);
    const float mw = L->stat[lane & 7][0], lw = L->stat[lane & 7][1];
    float ms = fmaxf(mw, dpp<XOR1>(mw)); ms = fmaxf(ms, dpp<XOR2>(ms)); ms = fmaxf(ms, dpp<XOR7>(ms));
    float ls = lw * __builtin_amdgcn_exp2f(mw - ms); ls += dpp<XOR1>(ls); ls += dpp<XOR2>(ls); ls += dpp<XOR7>(ls);
    if (lane < 2) __hip_atomic_store(slot + lane, ((u64)tag32 << 32) | __builtin_bit_cast(u32, lane == 0 ? ms : ls), RLX_AGENT);
    u64 got = 0;
    for (u32 spins = 0; spins < SPIN_CAP; ++spins) {
      got = __hip_atomic_load(oslot + (lane & 1), RLX_AGENT);
      if (__all((u32)(got >> 32) == tag32)) break;
      __builtin_amdgcn_s_sleep(64);
    }
    const float ov = __builtin_bit_cast(float, (u32)got);
    const float mo = rdlane(ov, 0), lo = rdlane(ov, 1);
    const float M = fmaxf(ms, mo);
    const float Lsum = ls * __builtin_amdgcn_exp2f(ms - M) + lo * __builtin_amdgcn_exp2f(mo - M);
    if (lane < 8) L->fac[lane] = L->gate * __builtin_amdgcn_exp2f(mw - M) / Lsum;
  }
  }
  wg_barrier();
  {
    const int d = min((wave < NS ? 128 + 16 * wave : 32 * (wave - NS)) + c, DH - 1);
    float v = (L->fac[4 * hf] * L->ow[4 * hf][d] + L->fac[4 * hf + 1] * L->ow[4 * hf + 1][d])
            + (L->fac[4 * hf + 2] * L->ow[4 * hf + 2][d] + L->fac[4 * hf + 3] * L->ow[4 * hf + 3][d]);
    v = swap32_add(v, v);
    const u32 pk = cvtpk(v, dpp<XOR1>(v));
#pragma unroll
    for (int i = 0; i < 8; ++i) {
      const u32 op = __builtin_amdgcn_readlane(pk, 2 * i);
#pragma unroll
      for (int k = 0; k < 8; ++k) yacc[k] = dot2(wp[i][k], op, yacc[k]);
    }
    if (wave >= NS) {
#pragma unroll
      for (int i = 8; i < 16; ++i) {
        const u32 op = __builtin_amdgcn_readlane(pk, 2 * i);
#pragma unroll
        for (int k = 0; k < 8; ++k) yacc[k] = dot2(wp[i][k], op, yacc[k]);
      }
    }
  }
  if (lane < 56) {
    *(LAS f32x4*)&L->yw[wave][8 * lane] = f32x4{yacc[0], yacc[1], yacc[2], yacc[3]};
    *(LAS f32x4*)&L->yw[wave][8 * lane + 4] = f32x4{yacc[4], yacc[5], yacc[6], yacc[7]};
  }
  wg_barrier();
  if (wave >= 7) return;
  {
    const u64 tg = uni64(L->tag);
    float sm = 0.f;
#pragma unroll
    for (int k = 0; k < NWAVE; ++k) sm += L->yw[k][tid];
    GAS u64* ys = (GAS u64*)(ws + WS_Y);
    __hip_atomic_store(ys + (size_t)chunk * DM + tid, pack24(tg, sm), RLX_AGENT);
    const int col = CPW * j + 2 * wave + hf;
    GAS u64* src = ys + (size_t)(b * GRP + c) * DM + col;
    u64 got = 0;
    for (u32 spins = 0; spins < SPIN_CAP; ++spins) {
      got = __hip_atomic_load(src, RLX_AGENT);
      if (__all(ok24(tg, got))) break;
      __builtin_amdgcn_s_sleep(2);
    }
    const float tot = sum32(val24(got));
    if (c == 0) out[b * DM + col] = (unsigned short)(cvtpk(tot, tot) & 0xffffu);
    if (blk == 0 && tid == 0) {
      GAS u64* ep = (GAS u64*)(ws + WS_EPOCH);
      __hip_atomic_store(ep, __hip_atomic_load(ep, RLX_AGENT) + 1, RLX_AGENT);
    }
  }
}

}

extern "C" void kernel_launch(void* const* d_in, const int* in_sizes, int n_in, void* d_out, int out_size, void* d_ws, size_t ws_size,
                              hipStream_t stream) {
  (void)in_sizes; (void)n_in; (void)out_size; (void)ws_size;
  hipLaunchKernelGGL(fused_decode, dim3(NWG), dim3(NTHR), 0, stream,
                     (const u32*)d_in[0], (const u32*)d_in[1], (const u32*)d_in[2], (const u32*)d_in[3], (const u32*)d_in[4],
                     (const u32*)d_in[6], (const u32*)d_in[7], (const u32*)d_in[8], (const u32*)d_in[9],
                     (unsigned short*)d_out, (unsigned char*)d_ws);
}
```
